```python
import torch

def _softmax(x, axis=-1):
    return torch.softmax(x, dim=axis)
def _gelu(x):
    return torch.nn.functional.gelu(x, approximate="tanh")
def _silu(x):
    return torch.nn.functional.silu(x)
def _sigmoid(x):
    return torch.sigmoid(x)
def _layernorm(x, axis=-1, eps=1e-5):
    m = x.mean(dim=axis, keepdim=True)
    v = x.var(dim=axis, keepdim=True, unbiased=False)
    return (x - m) / torch.sqrt(v + eps)
def _scatter_add(tgt, idx, src, axis=0):
    return tgt.clone().index_add_(axis, idx, src)
def _segment_sum(src, seg, n):
    out = torch.zeros((n,) + src.shape[1:], dtype=src.dtype)
    return out.index_add_(0, seg, src)
def _pad(x, pw):
    flat = tuple(v for pair in reversed(pw) for v in pair)
    return torch.nn.functional.pad(x, flat)

kernel_name = 'interleave_layers__sliding_block_cached__dense_activation_gelu__decode'

def setup_inputs():

    _w = (lambda a: (lambda t: t.to(torch.bfloat16) if t.is_floating_point() else t)(torch.as_tensor(a)))
    def _A_setup():
        B, Sq, D = 1, 1, 512
        Hq, Hkv, Sk, Dh = 32, 8, 65536, 128
        assert Hq % Hkv == 0, (Hq, Hkv)
        return {
            "x": _w((lambda *s: torch.randn(*s, dtype=torch.bfloat16) * 0.05)(B, Sq, D)),
            "k_cache": _w((lambda *s: torch.randn(*s, dtype=torch.bfloat16) * 0.8)(B, Hkv, Sk, Dh)),
            "v_cache": _w((lambda *s: torch.randn(*s, dtype=torch.bfloat16) * 1.6)(B, Hkv, Sk, Dh)),
            "wq": _w((lambda *s: torch.randn(*s, dtype=torch.bfloat16) * 0.8)(D, Hq * Dh) * (D ** -0.5)),
            "wo": _w((lambda *s: torch.randn(*s, dtype=torch.bfloat16) * 1.6)(Hq * Dh, D) * ((Hq * Dh) ** -0.5)),
        }
    _A_in = _A_setup()

    def _B_setup():
        B, S, D, F = 1, 1, 512, 1792
        return {
            "x": _w((lambda *s: torch.randn(*s, dtype=torch.bfloat16) * 0.05)(B, S, D)),
            "w1": _w((lambda *s: torch.randn(*s, dtype=torch.bfloat16) * 1.6)(D, F) * (D ** -0.5)),
            "b1": _w((lambda *s: torch.randn(*s, dtype=torch.bfloat16) * 1.6)(F) * 0.01),
            "w2": _w((lambda *s: torch.randn(*s, dtype=torch.bfloat16) * 0.2)(F, D) * (F ** -0.5)),
            "b2": _w((lambda *s: torch.randn(*s, dtype=torch.bfloat16) * 0.2)(D) * 0.01),
        }
    _B_in = _B_setup()

    return {
        "A_x": _A_in["x"],
        "A_k_cache": _A_in["k_cache"],
        "A_v_cache": _A_in["v_cache"],
        "A_wq": _A_in["wq"],
        "A_wo": _A_in["wo"],
        "B_x": _B_in["x"],
        "B_w1": _B_in["w1"],
        "B_b1": _B_in["b1"],
        "B_w2": _B_in["w2"],
        "B_b2": _B_in["b2"],
    }

def reference(A_x, A_k_cache, A_v_cache, A_wq, A_wo, B_x, B_w1, B_b1, B_w2, B_b2):
    def _A_ref(x, k_cache, v_cache, wq, wo):
        B, Sq, D = x.shape
        Hq, Dh = 32, 128
        Hkv, Sk = k_cache.shape[1], k_cache.shape[2]
        W = min(4096, max(256, Sk // 4))
        rep = Hq // Hkv
        q = (lambda x, *d: x.permute(*d))((x @ wq).reshape(B, Sq, Hq, Dh), 0, 2, 1, 3)
        k = (lambda x, n, axis: torch.repeat_interleave(x, n, dim=axis))(k_cache[:, :, Sk - W:, :], rep, axis=1)
        v = (lambda x, n, axis: torch.repeat_interleave(x, n, dim=axis))(v_cache[:, :, Sk - W:, :], rep, axis=1)
        scores = torch.einsum("bhqd,bhkd->bhqk", q, k) * (Dh ** -0.5)
        p = _softmax(scores, axis=-1)
        o = torch.einsum("bhqk,bhkd->bhqd", p, v)
        return (lambda x, *d: x.permute(*d))(o, 0, 2, 1, 3).reshape(B, Sq, Hq * Dh) @ wo

    def _B_ref(x, w1, b1, w2, b2):
        h = torch.einsum("bsd,df->bsf", x, w1) + b1
        h = _gelu(h)
        return torch.einsum("bsf,fd->bsd", h, w2) + b2

    x = A_x
    x = x + _A_ref(_layernorm(x, axis=-1), A_k_cache, A_v_cache, A_wq, A_wo)
    x = x + _B_ref(_layernorm(x, axis=-1), B_w1, B_b1, B_w2, B_b2)
    x = x + _A_ref(_layernorm(x, axis=-1), A_k_cache, A_v_cache, A_wq, A_wo)
    x = x + _B_ref(_layernorm(x, axis=-1), B_w1, B_b1, B_w2, B_b2)
    return x
```

```cpp
#define DRY_A 0
#define DRY_Q 0
#define DRY_M 0
#include <hip/hip_runtime.h>
#include <stdint.h>

typedef unsigned short u16;
typedef unsigned long long u64;
typedef unsigned v4u __attribute__((ext_vector_type(4)));
typedef unsigned v2u __attribute__((ext_vector_type(2)));
typedef float f32x4 __attribute__((ext_vector_type(4)));
typedef short bf16x8 __attribute__((ext_vector_type(8)));
typedef __bf16 bf16x2 __attribute__((ext_vector_type(2)));

#define STAMPS 0

namespace {

constexpr int SK = 65536, DH = 128, WIN = 4096, FF = 1792;
constexpr int NT = 512;
#define MLP_DELAY 1

constexpr unsigned OFF_EPOCH = 0;
constexpr unsigned OFF_FLAGS = 4096;
constexpr unsigned OFF_MB = 65536;
constexpr unsigned OFF_QP = OFF_MB;
constexpr unsigned OFF_CS = OFF_QP + 65536;
constexpr unsigned OFF_QS = OFF_CS + 32768;
constexpr unsigned OFF_X2 = OFF_QS + 8192;
constexpr unsigned OFF_X1 = OFF_X2 + 2048;
constexpr unsigned OFF_SG = OFF_X1 + 4096;
constexpr unsigned OFF_P2 = OFF_MB + 131072;
constexpr unsigned OFF_XP = OFF_P2;
constexpr unsigned OFF_ML = OFF_XP + 32768;
constexpr unsigned OFF_YP = OFF_ML + 32768;
constexpr unsigned OFF_AP = OFF_YP + 131072;
constexpr unsigned OFF_END = OFF_AP + 524288;
static_assert(OFF_SG + 4096 <= OFF_P2 && OFF_END <= OFF_P2 + 32u * 3u * 8192u, "mailboxes exceed the cleared regions");
constexpr unsigned WS_RANGE = OFF_P2 + 32u * 3u * 8192u;
constexpr unsigned OOB = 0x7ffffff0u;
constexpr unsigned OFF_STAMP = 4u << 20;
constexpr unsigned SPIN_CAP = 1u << 22;
constexpr unsigned INVALID = 0xffffffffu;

constexpr int LDS_SMALL = 128 * 1024;
constexpr int LDS_BYTES = LDS_SMALL + 8192 + (STAMPS ? 2048 : 0);

__device__ __forceinline__ float bflo(unsigned w) { return __uint_as_float(w << 16); }
__device__ __forceinline__ float bfhi(unsigned w) { return __uint_as_float(w & 0xffff0000u); }
__device__ __forceinline__ unsigned pack2(float lo, float hi) {
    bf16x2 v = {(__bf16)lo, (__bf16)hi};
    return __builtin_bit_cast(unsigned, v);
}
__device__ __forceinline__ float rbf(float f) { return bflo(pack2(f, f)); }
__device__ __forceinline__ unsigned usafe(unsigned u) { return u < 0xfffffffeu ? u : 0xfffffffeu; }
__device__ __forceinline__ unsigned fsafe(float f) { return usafe(__float_as_uint(f)); }

template <int CTRL> __device__ __forceinline__ float dpp(float x) {
    return __builtin_bit_cast(float, __builtin_amdgcn_mov_dpp(__builtin_bit_cast(int, x), CTRL, 0xf, 0xf, true));
}
constexpr int XOR1 = 0xB1, XOR2 = 0x4E, XOR7 = 0x141, ROR8 = 0x128, ROR4 = 0x124, BCAST = 0x150;
__device__ __forceinline__ float tsum16(float a, float b) {
    auto s = __builtin_amdgcn_permlane16_swap(__float_as_uint(a), __float_as_uint(b), false, false);
    return __uint_as_float(s[0]) + __uint_as_float(s[1]);
}
__device__ __forceinline__ float tsum32(float a, float b) {
    auto s = __builtin_amdgcn_permlane32_swap(__float_as_uint(a), __float_as_uint(b), false, false);
    return __uint_as_float(s[0]) + __uint_as_float(s[1]);
}
__device__ __forceinline__ float sum16(float x) { return tsum16(x, x); }
__device__ __forceinline__ float sum32(float x) { return tsum32(x, x); }
__device__ __forceinline__ float max16(float x) {
    auto s = __builtin_amdgcn_permlane16_swap(__float_as_uint(x), __float_as_uint(x), false, false);
    return fmaxf(__uint_as_float(s[0]), __uint_as_float(s[1]));
}
__device__ __forceinline__ float max32(float x) {
    auto s = __builtin_amdgcn_permlane32_swap(__float_as_uint(x), __float_as_uint(x), false, false);
    return fmaxf(__uint_as_float(s[0]), __uint_as_float(s[1]));
}
__device__ __forceinline__ float wavesum(float x) {
    x += dpp<XOR1>(x); x += dpp<XOR2>(x); x += dpp<XOR7>(x); x += dpp<ROR8>(x); return sum32(sum16(x));
}
__device__ __forceinline__ float tsum4(f32x4 v) { return tsum32(tsum16(v[0], v[1]), tsum16(v[2], v[3])); }

__device__ __forceinline__ v4u ntload(const u16* p) { return __builtin_nontemporal_load((const v4u*)p); }
__device__ __forceinline__ v4u ld16(__amdgpu_buffer_rsrc_t rs, unsigned off) { return __builtin_amdgcn_raw_buffer_load_b128(rs, off, 0, 16); }
__device__ __forceinline__ v2u ld8(__amdgpu_buffer_rsrc_t rs, unsigned off) { return __builtin_amdgcn_raw_buffer_load_b64(rs, off, 0, 16); }
__device__ __forceinline__ unsigned ld4(__amdgpu_buffer_rsrc_t rs, unsigned off) { return __builtin_amdgcn_raw_buffer_load_b32(rs, off, 0, 16); }
__device__ __forceinline__ void st16(__amdgpu_buffer_rsrc_t rs, unsigned off, v4u v) { __builtin_amdgcn_raw_buffer_store_b128(v, rs, off, 0, 16); }
__device__ __forceinline__ void st8(__amdgpu_buffer_rsrc_t rs, unsigned off, v2u v) { __builtin_amdgcn_raw_buffer_store_b64(v, rs, off, 0, 16); }
__device__ __forceinline__ void st4(__amdgpu_buffer_rsrc_t rs, unsigned off, unsigned v) { __builtin_amdgcn_raw_buffer_store_b32(v, rs, off, 0, 16); }
__device__ __forceinline__ unsigned umax4(v4u v) { return max(max(v.x, v.y), max(v.z, v.w)); }
__device__ __forceinline__ f32x4 asf(v4u v) { return __builtin_bit_cast(f32x4, v); }
#define MEMBAR() asm volatile("" ::: "memory")

__device__ __forceinline__ bool gate_ok(v2u e, v2u f) {
    u64 tag = (((u64)e.y << 32) | e.x) + 1;
    return __all((((u64)f.y << 32) | f.x) == tag);
}
__device__ __forceinline__ void gate(__amdgpu_buffer_rsrc_t rs, int l) {
    for (unsigned spin = 0; spin < SPIN_CAP; ++spin) {
        MEMBAR();
        v2u e = ld8(rs, OFF_EPOCH);
        v2u f = ld8(rs, OFF_FLAGS + (unsigned)(l & 31) * 128u + (unsigned)(l >> 5) * 8u);
        if (gate_ok(e, f)) break;
        __builtin_amdgcn_s_sleep(2);
    }
    MEMBAR();
}
__device__ __forceinline__ void gate1(__amdgpu_buffer_rsrc_t rs, int l) {
    for (unsigned spin = 0; spin < SPIN_CAP; ++spin) {
        MEMBAR();
        v2u e = ld8(rs, OFF_EPOCH);
        v2u f = ld8(rs, OFF_FLAGS + (unsigned)(l & 31) * 128u);
        if (gate_ok(e, f)) break;
        __builtin_amdgcn_s_sleep(2);
    }
    MEMBAR();
}
__device__ __forceinline__ void wait_signal(__amdgpu_buffer_rsrc_t rs, unsigned off, int w) {
    if (w == 0) {
        for (unsigned spin = 0; spin < SPIN_CAP; ++spin) {
            MEMBAR();
            if (__builtin_amdgcn_readfirstlane(ld4(rs, off)) != INVALID) break;
        }
    }
    __syncthreads();
}
#define KEEP(x) asm volatile("" :: "v"(x))
#define BAD4(v) __any(umax4(v) == INVALID)
#define PIN8(a) asm volatile("" : "+v"(a[0]), "+v"(a[1]), "+v"(a[2]), "+v"(a[3]), "+v"(a[4]), "+v"(a[5]), "+v"(a[6]), "+v"(a[7]))

__device__ __forceinline__ void tr8_store(const v4u (&in)[8], unsigned char* stg, int C, int kp, int chunk) {
    unsigned char* base = stg + (size_t)(kp * C + 8 * chunk) * 16;
    const int sw = chunk & 7;
#pragma unroll
    for (int dw = 0; dw < 4; ++dw) {
        v4u lo, hi;
#pragma unroll
        for (int kk = 0; kk < 4; ++kk) {
            lo[kk] = __builtin_amdgcn_perm(in[2 * kk + 1][dw], in[2 * kk][dw], 0x05040100u);
            hi[kk] = __builtin_amdgcn_perm(in[2 * kk + 1][dw], in[2 * kk][dw], 0x07060302u);
        }
        *(v4u*)(base + (((2 * dw) ^ sw) << 4)) = lo;
        *(v4u*)(base + (((2 * dw + 1) ^ sw) << 4)) = hi;
    }
}
__device__ __forceinline__ v4u frag_load(const unsigned char* stg, int C, int kp, int ct, int n) {
    const int ch = 2 * ct + (n >> 3);
    return *(const v4u*)(stg + (size_t)(kp * C + 8 * ch + ((n & 7) ^ (ch & 7))) * 16);
}
__device__ __forceinline__ f32x4 mfma(v4u a, v4u b, f32x4 c) {
    return __builtin_amdgcn_mfma_f32_16x16x32_bf16(__builtin_bit_cast(bf16x8, a), __builtin_bit_cast(bf16x8, b), c, 0, 0, 0);
}
#define ZERO4 (f32x4{0.f, 0.f, 0.f, 0.f})

#define STAMP(idx) do { } while (0)
#define STAMPC(idx) do { } while (0)
#define STAMP_INIT() do { } while (0)
#define STAMP_DUMP() do { } while (0)

__global__ void __launch_bounds__(NT, 2)
fused_kernel(const u16* __restrict__ x0, const u16* __restrict__ kc, const u16* __restrict__ vc,
             const u16* __restrict__ wq, const u16* __restrict__ wo, const u16* __restrict__ w1,
             const u16* __restrict__ b1, const u16* __restrict__ w2, const u16* __restrict__ b2,
             u16* __restrict__ out, unsigned char* ws) {
    __shared__ __attribute__((aligned(16))) unsigned char lds[LDS_BYTES];
    const int b = blockIdx.x, xg = b & 7, bi = b >> 3;
    const int tid = threadIdx.x, w = __builtin_amdgcn_readfirstlane(tid >> 6), l = tid & 63, n = l & 15, g4 = l >> 4;
    const __amdgpu_buffer_rsrc_t rs = __builtin_amdgcn_make_buffer_rsrc(ws, 0, WS_RANGE, 0x00020000);
    unsigned char* sm = lds + LDS_SMALL;
    const v4u ONES = {0x3f803f80u, 0x3f803f80u, 0x3f803f80u, 0x3f803f80u};
    const v4u Z4 = {0u, 0u, 0u, 0u};
    STAMP_INIT(); STAMP(0); STAMPC(30);

    if (bi < 4) {
        const int c = bi * 8 + xg;
        v2u e = ld8(rs, OFF_EPOCH);
        const v4u inv = {INVALID, INVALID, INVALID, INVALID};
        if (w == 0) {
#pragma unroll
            for (int k = 0; k < 4; ++k) st16(rs, OFF_MB + (unsigned)(c * 4096 + k * 1024 + l * 16), inv);
            asm volatile("s_waitcnt vmcnt(0)" ::: "memory");
            const u64 tag = (((u64)e.y << 32) | e.x) + 1;
            if (l == 0) st8(rs, OFF_FLAGS + (unsigned)c * 128u, v2u{(unsigned)tag, (unsigned)(tag >> 32)});
            STAMP(1);
        }
#pragma unroll
        for (int k = 0; k < 3; ++k) st16(rs, OFF_P2 + (unsigned)((c * 3 + k) * 8192 + tid * 16), inv);
        asm volatile("s_waitcnt vmcnt(0)" ::: "memory");
        __syncthreads();
        if (tid == 0) {
            const u64 tag = (((u64)e.y << 32) | e.x) + 1;
            st8(rs, OFF_FLAGS + (unsigned)c * 128u + 8u, v2u{(unsigned)tag, (unsigned)(tag >> 32)});
        }
        STAMP(2); STAMP_DUMP();
        return;
    }

    if (bi < 12) {
        const int u = bi - 4, g = xg, ks = u >> 1, ns = 2 * g + (u & 1), cb = u;
        v4u Bq[2][4], Bo[16], in[8];
        const int xcl = 16 * w + 4 * (n & 3) + g4;
        const int xc = 128 * ks + xcl;
        unsigned x0r = x0[xc], b2r = b2[xc];
        float x0v = 0.f, b2v = 0.f;
        __builtin_amdgcn_sched_barrier(0);
        {
            const u16* p = wq + (size_t)(128 * ks + 32 * (w >> 1) + 8 * g4) * 4096 + 256 * ns + 128 * (w & 1) + 8 * n;
#pragma unroll
            for (int j = 0; j < 8; ++j) in[j] = ntload(p + (size_t)j * 4096);
            __builtin_amdgcn_sched_barrier(0);
        }
#pragma unroll
        for (int st = 0; st < 4; ++st) { Bq[0][st] = Z4; Bq[1][st] = Z4; }
#pragma unroll
        for (int st = 0; st < 16; ++st) Bo[st] = Z4;
        const bool qst = (g4 < 2) && !(n & 1);
        const unsigned qcol2 = (unsigned)(ks * 4096 + 256 * ns + 32 * w + 16 * (g4 & 1) + n) * 2u;
        float csv = 0.f;
        u16* xs = (u16*)sm;
        u16* os = (u16*)(sm + 1024);
        const unsigned yoff = l < 56 ? OFF_YP + (unsigned)((2 * (l >> 2)) * 512 + 128 * ks + 16 * w + 4 * (l & 3)) * 4u : OOB - 131072u;
        const unsigned aoff = OFF_AP + (unsigned)(g * 16 + 4 * g4) * 2048u + (unsigned)(16 * w + n) * 16u;
        const unsigned moff = OFF_ML + (unsigned)(g * 16 + (l >> 2)) * 128u + (unsigned)(n & 3) * 8u;
#pragma unroll 1
        for (int it = -DRY_Q; it < 3; ++it) {
            const bool live = it >= 0;
            if (it == 0) {
                PIN8(in); asm volatile("" : "+v"(x0r), "+v"(b2r));
                x0v = bflo(x0r); b2v = bflo(b2r);
                tr8_store(in, lds, 256, 4 * (w >> 1) + g4, 16 * (w & 1) + n);
                if (w == 0) gate1(rs, l);
                __syncthreads();
#pragma unroll
                for (int c = 0; c < 2; ++c)
#pragma unroll
                    for (int st = 0; st < 4; ++st) Bq[c][st] = frag_load(lds, 256, 4 * st + g4, 2 * w + c, n);
                STAMP(1);
            }
            if (it <= 0) {
                f32x4 a0 = ZERO4, a1 = ZERO4;
#pragma unroll
                for (int st = 0; st < 4; ++st) { a0 = mfma(ONES, Bq[0][st], a0); a1 = mfma(ONES, Bq[1][st], a1); }
                csv = (g4 & 1) ? a1[0] : a0[0];
                { const unsigned cv = usafe(pack2(csv, dpp<XOR1>(csv))); if (live) st4(rs, qst ? OFF_CS + qcol2 : OOB, cv); }
            }
            float xv = x0v;
            if (it != 0) {
                if (it == 2 && ns != 0) { STAMP_DUMP(); return; }
                v4u y0 = Z4, y1 = Z4; unsigned x1 = 0;
                if (live) {
                    const unsigned yo = yoff + (unsigned)(it - 1) * 65536u, xo = OFF_X1 + (unsigned)((it - 1) * 512 + xc) * 4u;
                    wait_signal(rs, OFF_X1 + (unsigned)(it - 1) * 2048u, w);
                    bool n0 = true, n1 = true, n2 = true;
                    for (unsigned spin = 0; spin < SPIN_CAP; ++spin) {
                        MEMBAR();
                        if (n0) y0 = ld16(rs, yo);
                        if (n1) y1 = ld16(rs, yo + 2048u);
                        if (n2) x1 = ld4(rs, xo);
                        n0 = BAD4(y0); n1 = BAD4(y1); n2 = __any(x1 == INVALID);
                        if (!(n0 | n1 | n2)) break;
                    }
                    if (it == 1 && u == 0 && tid == 0) st4(rs, OFF_SG + (unsigned)(16 + g) * 128u, 1u);
                    STAMP(3 + 8 * it);
                }
                float t = tsum4(asf(y0) + asf(y1));
                t += dpp<ROR4>(t); t += dpp<ROR8>(t);
                xv = rbf(__uint_as_float(x1) + rbf(rbf(t) + b2v));
                if (it == 2) {
                    if (n < 4) out[xc] = (u16)(__float_as_uint(xv) >> 16);
                    if (ks == 0 && tid == 0) {
                        v2u e = ld8(rs, OFF_EPOCH);
                        u64 tag = (((u64)e.y << 32) | e.x) + 1;
                        st8(rs, OFF_EPOCH, v2u{(unsigned)tag, (unsigned)(tag >> 32)});
                    }
                    STAMP(4 + 8 * it); STAMP_DUMP();
                    return;
                }
                if (live && ns == 0) st4(rs, n < 4 ? OFF_X2 + (unsigned)xc * 4u : OOB, fsafe(xv));
            }
            xs[xcl] = (u16)(__float_as_uint(xv) >> 16);
            __syncthreads();
            {
                f32x4 a0 = ZERO4, a1 = ZERO4;
                v4u A[4];
#pragma unroll
                for (int st = 0; st < 4; ++st) A[st] = *(const v4u*)(xs + 32 * st + 8 * g4);
                const unsigned xw = *(const unsigned*)(xs + 2 * l);
#pragma unroll
                for (int st = 0; st < 4; ++st) { a0 = mfma(A[st], Bq[0][st], a0); a1 = mfma(A[st], Bq[1][st], a1); }
                const float e0 = bflo(xw), e1 = bfhi(xw);
                const float mean = wavesum(e0 + e1) * (1.f / 128.f);
                const float qv = fmaf(-mean, csv, (g4 & 1) ? a1[0] : a0[0]);
                { const unsigned qw = usafe(pack2(qv, dpp<XOR1>(qv))); KEEP(qw); if (live) st4(rs, qst ? OFF_QP + (unsigned)it * 32768u + qcol2 : OOB, qw); }
                if (w == 0 && !(u & 1)) {
                    const float d0 = e0 - mean, d1 = e1 - mean;
                    const float m2 = wavesum(d0 * d0 + d1 * d1);
                    KEEP(m2); if (live) st8(rs, l == 0 ? OFF_QS + (unsigned)((it * 4 + ks) * 8 + g) * 128u : OOB, v2u{fsafe(mean), fsafe(m2)});
                }
            }
            if (live) STAMP(4 + 8 * it);
            if (it == 0) {
                const u16* p = wo + (size_t)(512 * g + 64 * w + 32 * (n >> 3) + 8 * g4) * 512 + 64 * cb + 8 * (n & 7);
#pragma unroll
                for (int j = 0; j < 8; ++j) in[j] = ntload(p + (size_t)j * 512);
                __builtin_amdgcn_sched_barrier(0);
                tr8_store(in, lds, 64, 8 * w + 4 * (n >> 3) + g4, n & 7);
                if (w == 0) gate(rs, l);
                __syncthreads();
#pragma unroll
                for (int st = 0; st < 16; ++st) Bo[st] = frag_load(lds, 64, 4 * st + g4, w & 3, n);
                STAMP(5);
            }
            {
                v4u a[4] = {Z4, Z4, Z4, Z4}; v2u ml = {0u, 0u};
                if (live) {
                    const unsigned ao = aoff + (unsigned)it * 262144u, mo = moff + (unsigned)it * 16384u;
                    wait_signal(rs, OFF_SG + (unsigned)(it * 8 + g) * 128u, w);
                    bool n0 = true, n1 = true, n2 = true, n3 = true, n4 = true;
                    for (unsigned spin = 0; spin < SPIN_CAP; ++spin) {
                        MEMBAR();
                        if (n4) ml = ld8(rs, mo);
                        if (n0) a[0] = ld16(rs, ao);
                        if (n1) a[1] = ld16(rs, ao + 2048u);
                        if (n2) a[2] = ld16(rs, ao + 4096u);
                        if (n3) a[3] = ld16(rs, ao + 6144u);
                        n0 = BAD4(a[0]); n1 = BAD4(a[1]); n2 = BAD4(a[2]); n3 = BAD4(a[3]); n4 = __any(max(ml.x, ml.y) == INVALID);
                        if (!(n0 | n1 | n2 | n3 | n4)) break;
                    }
                    if (b == 32 && tid == 0) st4(rs, OFF_SG + (unsigned)(24 + it) * 128u, 1u);
                    STAMP(6 + 8 * it);
                }
                const float mm = __uint_as_float(ml.x);
                float M = fmaxf(mm, dpp<ROR4>(mm)); M = fmaxf(M, dpp<ROR8>(M)); M = max32(max16(M));
                const float e = __builtin_amdgcn_exp2f(mm - M);
                float L = e * __uint_as_float(ml.y); L += dpp<ROR4>(L); L += dpp<ROR8>(L); L = sum32(sum16(L));
                const float al = e * __builtin_amdgcn_rcpf(L);
                f32x4 o;
                o[0] = dpp<BCAST + 0>(al) * asf(a[0])[0]; o[1] = dpp<BCAST + 1>(al) * asf(a[0])[1];
                o[2] = dpp<BCAST + 2>(al) * asf(a[0])[2]; o[3] = dpp<BCAST + 3>(al) * asf(a[0])[3];
                o[0] = fmaf(dpp<BCAST + 4>(al), asf(a[1])[0], o[0]); o[1] = fmaf(dpp<BCAST + 5>(al), asf(a[1])[1], o[1]);
                o[2] = fmaf(dpp<BCAST + 6>(al), asf(a[1])[2], o[2]); o[3] = fmaf(dpp<BCAST + 7>(al), asf(a[1])[3], o[3]);
                o[0] = fmaf(dpp<BCAST + 8>(al), asf(a[2])[0], o[0]); o[1] = fmaf(dpp<BCAST + 9>(al), asf(a[2])[1], o[1]);
                o[2] = fmaf(dpp<BCAST + 10>(al), asf(a[2])[2], o[2]); o[3] = fmaf(dpp<BCAST + 11>(al), asf(a[2])[3], o[3]);
                o[0] = fmaf(dpp<BCAST + 12>(al), asf(a[3])[0], o[0]); o[1] = fmaf(dpp<BCAST + 13>(al), asf(a[3])[1], o[1]);
                o[2] = fmaf(dpp<BCAST + 14>(al), asf(a[3])[2], o[2]); o[3] = fmaf(dpp<BCAST + 15>(al), asf(a[3])[3], o[3]);
                const float ov = tsum4(o);
                os[128 * g4 + 16 * w + n] = (u16)pack2(ov, ov);
                __syncthreads();
                if (w < 4) {
                    f32x4 acc = ZERO4;
#pragma unroll
                    for (int st = 0; st < 16; ++st) acc = mfma(*(const v4u*)(os + 32 * st + 8 * g4), Bo[st], acc);
                    KEEP(acc[0]); if (live) st4(rs, g4 == 0 ? OFF_XP + (unsigned)((it * 8 + g) * 512 + 64 * cb + 16 * w + n) * 4u : OOB, fsafe(acc[0]));
                }
                if (live) STAMP(7 + 8 * it); else STAMP(2);
            }
        }
        return;
    }

    if (bi < 28) {
        const int g = xg, s = bi - 12;
        const size_t base = ((size_t)g * SK + (SK - WIN) + 256 * s + 32 * w) * DH + g4 * DH + 8 * n;
        v4u Kf[2][4], Vf[8], Vr[8];
#pragma unroll
        for (int jj = 0; jj < 8; ++jj) Vr[jj] = ntload(vc + base + 4 * jj * DH);
        __builtin_amdgcn_sched_barrier(0);
        STAMP(22);
#pragma unroll
        for (int st = 0; st < 4; ++st) { Kf[0][st] = Z4; Kf[1][st] = Z4; }
#pragma unroll
        for (int kb = 0; kb < 8; ++kb) Vf[kb] = Z4;
        float* mxs = (float*)sm;
        float* lss = (float*)(sm + 256);
        v2u* qss = (v2u*)(sm + 512);
        unsigned* Ps = (unsigned*)(sm + 1024);
        unsigned char* qim = lds;
        const int h = n >> 2;
        const bool lead = (g4 == 0) && ((n & 3) == 0);
        const int sidx = lead ? h * 8 + w : 40;
        const int pidx = ((w * 4 + h) * 4 + g4) * 4 + 2 * (n & 1) + ((n >> 1) & 1);
        const unsigned goff = (w < 4 ? OFF_QP : OFF_CS) + (unsigned)((w & 3) * 4096 + 512 * g + 8 * l) * 2u;
        const unsigned soff = OFF_QS + (unsigned)((w & 3) * 8 + g) * 128u;
        const int gw = ((w >> 2) * 16 + (w & 3) * 4 + g4) * 272 + 16 * n;
        const int gr = ((n & 3) * 4 + h) * 272 + 16 * g4;
        f32x4 KC[2] = {ZERO4, ZERO4};
#pragma unroll 1
        for (int it = -DRY_A; it < 2; ++it) {
            const bool live = it >= 0;
            if (!live) STAMP(23);
            if (it == 0) {
                v4u Kr[8];
#pragma unroll
                for (int jj = 0; jj < 8; ++jj) Kr[jj] = ntload(kc + base + 4 * jj * DH);
                __builtin_amdgcn_sched_barrier(0);
                PIN8(Vr);
                tr8_store(Vr, lds, 128, 4 * w + g4, n);
                STAMP(5);
                v2u ge = {0u, 0u}, gf = {0u, 0u};
                if (w == 0) { MEMBAR(); ge = ld8(rs, OFF_EPOCH); gf = ld8(rs, OFF_FLAGS + (unsigned)(l & 31) * 128u + (unsigned)(l >> 5) * 8u); }
                __builtin_amdgcn_sched_barrier(0);
                unsigned char* kst = lds + 65536 + w * 8192;
#pragma unroll
                for (int jj = 0; jj < 8; ++jj) *(v4u*)(kst + (4 * jj + g4) * 256 + ((n ^ (4 * g4 + (jj & 3))) << 4)) = Kr[jj];
                STAMP(6);
                if (w == 0) { if (!gate_ok(ge, gf)) gate(rs, l); }
                __syncthreads();
#pragma unroll
                for (int kb = 0; kb < 8; ++kb) Vf[kb] = frag_load(lds, 128, 4 * kb + g4, w, n);
#pragma unroll
                for (int t = 0; t < 2; ++t)
#pragma unroll
                    for (int st = 0; st < 4; ++st) Kf[t][st] = *(const v4u*)(kst + (16 * t + 4 * (n & 3) + (n >> 2)) * 256 + (((4 * st + g4) ^ n) << 4));
                __syncthreads();
                STAMP(1);
            }
            {
                v4u qd = Z4; v2u qs = {0u, 0u};
                if (live) {
                    if (it == 1) wait_signal(rs, OFF_SG + (unsigned)(16 + g) * 128u, w);
                    if (w < 4 || it == 0) {
                        const unsigned go = goff + (unsigned)it * 32768u, so = w < 4 ? soff + (unsigned)it * 4096u : OOB;
                        for (unsigned spin = 0; spin < SPIN_CAP; ++spin) {
                            MEMBAR();
                            qd = ld16(rs, go); qs = ld8(rs, so);
                            if (!__any(max(umax4(qd), max(qs.x, qs.y)) == INVALID)) break;
                        }
                    }
                    if (s == 0 && tid == 0) st4(rs, OFF_SG + (unsigned)(it * 8 + g) * 128u, 1u);
                    STAMP(3 + 8 * it);
                }
                if (w < 4 || it <= 0) *(v4u*)(qim + gw) = qd;
                if (w < 4 && l == 0) qss[w] = qs;
            }
            __syncthreads();
            if (it == 1) STAMPC(16); if (!live) STAMP(24);
            v4u Q[4];
            if (it <= 0) {
#pragma unroll
                for (int st = 0; st < 4; ++st) Q[st] = *(const v4u*)(qim + 16 * 272 + gr + 64 * st);
                KC[0] = ZERO4; KC[1] = ZERO4;
#pragma unroll
                for (int st = 0; st < 4; ++st) { KC[0] = mfma(Kf[0][st], Q[st], KC[0]); KC[1] = mfma(Kf[1][st], Q[st], KC[1]); }
            }
#pragma unroll
            for (int st = 0; st < 4; ++st) Q[st] = *(const v4u*)(qim + gr + 64 * st);
            const v2u qs = qss[n & 3];
            f32x4 D0 = ZERO4, D1 = ZERO4;
#pragma unroll
            for (int st = 0; st < 4; ++st) { D0 = mfma(Kf[0][st], Q[st], D0); D1 = mfma(Kf[1][st], Q[st], D1); }
            const float mk = __uint_as_float(qs.x);
            float mean = mk + dpp<XOR1>(mk); mean += dpp<XOR2>(mean); mean *= 0.25f;
            const float dm = mk - mean;
            float m2 = fmaf(128.f * dm, dm, __uint_as_float(qs.y)); m2 += dpp<XOR1>(m2); m2 += dpp<XOR2>(m2);
            const float delta = mk - rbf(mean);
            const float sc = __builtin_amdgcn_rcpf(rbf(__builtin_amdgcn_sqrtf(rbf(rbf(m2 * (1.f / 512.f)) + 1e-5f)))) * (0.08838834764831845f * 1.4426950408889634f);
            float y[4];
#pragma unroll
            for (int r = 0; r < 4; ++r) {
                const float x0_ = fmaf(delta, KC[0][r], D0[r]), x1_ = fmaf(delta, KC[1][r], D1[r]);
                const float s0 = x0_ + dpp<XOR1>(x0_), s1 = x1_ + dpp<XOR1>(x1_);
                y[r] = (n & 1) ? s1 : s0;
            }
            float z[2];
#pragma unroll
            for (int i = 0; i < 2; ++i) {
                const float s0 = y[i] + dpp<XOR2>(y[i]), s1 = y[2 + i] + dpp<XOR2>(y[2 + i]);
                z[i] = (n & 2) ? s1 : s0;
            }
            float mx = fmaxf(z[0], z[1]);
            mx = fmaxf(mx, dpp<XOR1>(mx)); mx = fmaxf(mx, dpp<XOR2>(mx)); mx = max32(max16(mx));
            mxs[sidx] = mx;
            if (it == 1) STAMPC(17);
            __syncthreads();
            if (it == 1) STAMPC(18); if (!live) STAMP(25);
            const f32x4 ma = *(const f32x4*)(mxs + h * 8), mb_ = *(const f32x4*)(mxs + h * 8 + 4);
            const float M = fmaxf(fmaxf(fmaxf(ma[0], ma[1]), fmaxf(ma[2], ma[3])), fmaxf(fmaxf(mb_[0], mb_[1]), fmaxf(mb_[2], mb_[3])));
            const float Msc = M * sc;
            const float p0 = __builtin_amdgcn_exp2f(fmaf(z[0], sc, -Msc)), p1 = __builtin_amdgcn_exp2f(fmaf(z[1], sc, -Msc));
            Ps[pidx] = pack2(p0, p1);
            float ls = p0 + p1; ls += dpp<XOR1>(ls); ls += dpp<XOR2>(ls); ls = sum32(sum16(ls));
            lss[sidx] = ls;
            if (it == 1) STAMPC(19);
            __syncthreads();
            if (it == 1) STAMPC(20); if (!live) STAMP(26);
            f32x4 acc = ZERO4;
#pragma unroll
            for (int kb = 0; kb < 8; ++kb) acc = mfma(*(const v4u*)(Ps + ((kb * 4 + (n & 3)) * 4 + g4) * 4), Vf[kb], acc);
            const unsigned rec = (unsigned)((it * 8 + g) * 16 + s);
            if (w == 7) {
                const f32x4 la = *(const f32x4*)(lss + h * 8), lb = *(const f32x4*)(lss + h * 8 + 4);
                const float L = ((la[0] + la[1]) + (la[2] + la[3])) + ((lb[0] + lb[1]) + (lb[2] + lb[3]));
                KEEP(L); if (live) st8(rs, lead ? OFF_ML + rec * 128u + (unsigned)h * 8u : OOB, v2u{fsafe(Msc), fsafe(L)});
            }
            KEEP(acc); if (live) st16(rs, g4 == 0 ? OFF_AP + rec * 2048u + (unsigned)(16 * w + n) * 16u : OOB, v4u{fsafe(acc[0]), fsafe(acc[1]), fsafe(acc[2]), fsafe(acc[3])});
            if (it == 1) STAMPC(21);
            if (live) STAMP(4 + 8 * it); else { __syncthreads(); STAMP(2); }
        }
        STAMP_DUMP();
        return;
    }

    {
        const int p = (bi - 28) * 8 + xg;
        if (p >= 28) return;
        v4u B1[16], B2[4][2];
        u16* xs = (u16*)sm;
        u16* hs = (u16*)(sm + 1024);
        volatile __attribute__((address_space(3))) u64* stt = (volatile __attribute__((address_space(3))) u64*)(sm + 2048);
        if (tid < 3) stt[tid] = ~0ull;
#pragma unroll
        for (int st = 0; st < 16; ++st) B1[st] = Z4;
#pragma unroll
        for (int c = 0; c < 4; ++c) { B2[c][0] = Z4; B2[c][1] = Z4; }
        const int xc = 64 * w + 4 * n + g4;
        const int hc = 16 * (w & 3) + n;
        unsigned x0r = x0[xc], b1r = b1[64 * p + hc];
        float x0v = 0.f, b1v = 0.f;
        float cs1 = 0.f;
        const unsigned xoff = OFF_XP + (unsigned)(2 * g4 * 512 + 64 * w + 4 * n) * 4u;
#pragma unroll 1
        for (int it = -DRY_M; it < 2; ++it) {
            const bool live = it >= 0;
            if (!live) STAMP(23);
            if (it == 0) {
                if (w == 0) gate(rs, l);
                __syncthreads();
                STAMP(1);
                __builtin_amdgcn_s_sleep(MLP_DELAY);
                v4u in[8], in2[8];
                asm volatile("" : "+v"(x0r), "+v"(b1r));
                x0v = bflo(x0r); b1v = bflo(b1r);
                const u16* p1 = w1 + (size_t)(64 * w + 32 * (n >> 3) + 8 * g4) * FF + 64 * p + 8 * (n & 7);
#pragma unroll
                for (int j = 0; j < 8; ++j) in[j] = ntload(p1 + (size_t)j * FF);
                const u16* p2 = w2 + (size_t)(64 * p + 32 * (w >> 2) + 8 * g4) * 512 + 128 * (w & 3) + 8 * n;
#pragma unroll
                for (int j = 0; j < 8; ++j) in2[j] = ntload(p2 + (size_t)j * 512);
                __builtin_amdgcn_sched_barrier(0);
                tr8_store(in, lds, 64, 8 * w + 4 * (n >> 3) + g4, n & 7);
                tr8_store(in2, lds + 65536, 512, 4 * (w >> 2) + g4, 16 * (w & 3) + n);
                __syncthreads();
#pragma unroll
                for (int st = 0; st < 16; ++st) B1[st] = frag_load(lds, 64, 4 * st + g4, w & 3, n);
#pragma unroll
                for (int c = 0; c < 4; ++c)
#pragma unroll
                    for (int st = 0; st < 2; ++st) B2[c][st] = frag_load(lds + 65536, 512, 4 * st + g4, 4 * w + c, n);
                f32x4 a = ZERO4;
#pragma unroll
                for (int st = 0; st < 16; ++st) a = mfma(ONES, B1[st], a);
                cs1 = a[0];
                STAMP(5);
            }
            v4u a0 = Z4, a1 = Z4; unsigned xp = 0;
            if (live) {
                const unsigned xo = xoff + (unsigned)it * 16384u, po = it == 0 ? OOB : OFF_X2 + (unsigned)xc * 4u;
                wait_signal(rs, OFF_SG + (unsigned)(24 + it) * 128u, w);
                bool n0 = true, n1 = true, n2 = true;
                for (unsigned spin = 0; spin < SPIN_CAP; ++spin) {
                    MEMBAR();
                    if (n0) a0 = ld16(rs, xo);
                    if (n1) a1 = ld16(rs, xo + 2048u);
                    if (n2) xp = ld4(rs, po);
                    n0 = BAD4(a0); n1 = BAD4(a1); n2 = __any(xp == INVALID);
                    if (!(n0 | n1 | n2)) break;
                }
                STAMP(3 + 8 * it);
            }
            const float xprev = it == 1 ? __uint_as_float(xp) : x0v;
            const float xv = rbf(xprev + rbf(tsum4(asf(a0) + asf(a1))));
            xs[xc] = (u16)(__float_as_uint(xv) >> 16);
            if (p == w && live) st4(rs, OFF_X1 + (unsigned)(it * 512 + xc) * 4u, fsafe(xv));
            __syncthreads();
            if (w < 4) {
                f32x4 acc = ZERO4;
#pragma unroll
                for (int st = 0; st < 16; ++st) acc = mfma(*(const v4u*)(xs + 32 * st + 8 * g4), B1[st], acc);
                u64 sv;
                do { sv = stt[it + 1]; } while ((unsigned)(sv >> 32) == INVALID);
                const float mb = __uint_as_float((unsigned)sv), rden = __uint_as_float((unsigned)(sv >> 32));
                const float hv = rbf(rbf(fmaf(-mb, cs1, acc[0]) * rden) + b1v);
                const float uu = hv * fmaf(hv * hv, 0.044715f * 2.302208198f, 2.302208198f);
                const float gl = hv - hv * __builtin_amdgcn_rcpf(__builtin_amdgcn_exp2f(uu) + 1.f);
                hs[hc] = (u16)pack2(gl, gl);
            } else if (w == 4) {
                const v4u xq = *(const v4u*)(xs + 8 * l);
                float e[8];
#pragma unroll
                for (int k = 0; k < 4; ++k) { e[2 * k] = bflo(xq[k]); e[2 * k + 1] = bfhi(xq[k]); }
                const float mean = wavesum(((e[0] + e[1]) + (e[2] + e[3])) + ((e[4] + e[5]) + (e[6] + e[7]))) * (1.f / 512.f);
                float q = 0.f;
#pragma unroll
                for (int k = 0; k < 8; ++k) { const float d = e[k] - mean; q = fmaf(d, d, q); }
                const float var = wavesum(q) * (1.f / 512.f);
                const float rden = __builtin_amdgcn_rcpf(rbf(__builtin_amdgcn_sqrtf(rbf(rbf(var) + 1e-5f))));
                if (l == 0) stt[it + 1] = ((u64)fsafe(rden) << 32) | __float_as_uint(rbf(mean));
            }
            __syncthreads();
            {
                const v4u H0 = *(const v4u*)(hs + 8 * g4), H1 = *(const v4u*)(hs + 32 + 8 * g4);
                f32x4 acc[4];
#pragma unroll
                for (int c = 0; c < 4; ++c) acc[c] = mfma(H0, B2[c][0], ZERO4);
#pragma unroll
                for (int c = 0; c < 4; ++c) acc[c] = mfma(H1, B2[c][1], acc[c]);
                const float lo = (g4 & 1) ? acc[1][0] : acc[0][0], hi = (g4 & 1) ? acc[3][0] : acc[2][0];
                const float yv = (g4 & 2) ? hi : lo; KEEP(yv); if (live) st4(rs, OFF_YP + (unsigned)((it * 32 + p) * 512 + 64 * w + l) * 4u, fsafe(yv));
            }
            if (live) STAMP(4 + 8 * it); else STAMP(2);
        }
        STAMP_DUMP();
    }
}

}

extern "C" void kernel_launch(void* const* d_in, const int* in_sizes, int n_in,
                              void* d_out, int out_size, void* d_ws, size_t ws_size,
                              hipStream_t stream) {
    fused_kernel<<<256, NT, 0, stream>>>((const u16*)d_in[0], (const u16*)d_in[1], (const u16*)d_in[2],
                                         (const u16*)d_in[3], (const u16*)d_in[4], (const u16*)d_in[6],
                                         (const u16*)d_in[7], (const u16*)d_in[8], (const u16*)d_in[9],
                                         (u16*)d_out, (unsigned char*)d_ws);
}
```

```cpp
#include <hip/hip_runtime.h>
#include <stdint.h>

typedef unsigned short u16;
typedef unsigned long long u64;
typedef unsigned v4u __attribute__((ext_vector_type(4)));
typedef unsigned v2u __attribute__((ext_vector_type(2)));
typedef float f32x4 __attribute__((ext_vector_type(4)));
typedef short bf16x8 __attribute__((ext_vector_type(8)));
typedef __bf16 bf16x2 __attribute__((ext_vector_type(2)));

namespace {

constexpr int SK = 65536, DH = 128, WIN = 4096, FF = 1792;
constexpr int NT = 512;

constexpr int D_G1 = 4400;
constexpr int D_GA = 5600;
constexpr int D_Q1 = 12000;
constexpr int D_WO = 8500;
constexpr int D_ML = 9500;
constexpr int C_Q = 94, C_A = 80, C_W = 96, C_M = 122;
constexpr int RETRY = 4;
constexpr int KSPLIT = 4;

constexpr unsigned OFF_EPOCH = 0;
constexpr unsigned OFF_FLAGS = 4096;
constexpr unsigned OFF_MB = 65536;
constexpr unsigned OFF_QP = OFF_MB;
constexpr unsigned OFF_CS = OFF_QP + 65536;
constexpr unsigned OFF_QS = OFF_CS + 32768;
constexpr unsigned OFF_X2 = OFF_QS + 2048;
constexpr unsigned OFF_X1 = OFF_X2 + 2048;
constexpr unsigned OFF_P2 = OFF_MB + 131072;
constexpr unsigned OFF_XP = OFF_P2;
constexpr unsigned OFF_ML = OFF_XP + 16384;
constexpr unsigned OFF_YP = OFF_ML + 8192;
constexpr unsigned OFF_SA = OFF_YP + 65536;
constexpr unsigned OFF_SQ = OFF_SA + 16384;
constexpr unsigned OFF_SW = OFF_SQ + 4096;
constexpr unsigned OFF_SM = OFF_SW + 2048;
constexpr unsigned OFF_SR = OFF_SM + 1024;
constexpr unsigned OFF_AP = OFF_P2 + 131072;
constexpr unsigned OFF_END = OFF_AP + 262144;
constexpr unsigned OFF_AFLAGS = 16384;
static_assert(OFF_X1 + 4096 <= OFF_P2 && OFF_SR + 1024 <= OFF_AP, "mailboxes exceed the cleared regions");
constexpr unsigned WS_RANGE = OFF_END;
constexpr unsigned OOB = 0x7ffffff0u;
constexpr unsigned SPIN_CAP = 1u << 20;
constexpr unsigned INVALID = 0xffffffffu;

constexpr int LDS_STG = 20480;
constexpr int LDS_BYTES = LDS_STG + 128 * 1024;

__device__ __forceinline__ float bflo(unsigned w) { return __uint_as_float(w << 16); }
__device__ __forceinline__ float bfhi(unsigned w) { return __uint_as_float(w & 0xffff0000u); }
__device__ __forceinline__ unsigned pack2(float lo, float hi) {
    bf16x2 v = {(__bf16)lo, (__bf16)hi};
    return __builtin_bit_cast(unsigned, v);
}
__device__ __forceinline__ float rbf(float f) { return bflo(pack2(f, f)); }
__device__ __forceinline__ unsigned usafe(unsigned u) { return u < 0xfffffffeu ? u : 0xfffffffeu; }
__device__ __forceinline__ unsigned fsafe(float f) { return usafe(__float_as_uint(f)); }

template <int CTRL> __device__ __forceinline__ float dpp(float x) {
    return __builtin_bit_cast(float, __builtin_amdgcn_mov_dpp(__builtin_bit_cast(int, x), CTRL, 0xf, 0xf, true));
}
constexpr int XOR1 = 0xB1, XOR2 = 0x4E, XOR7 = 0x141, ROR8 = 0x128, ROR4 = 0x124, BCAST = 0x150;
__device__ __forceinline__ float tsum16(float a, float b) {
    auto s = __builtin_amdgcn_permlane16_swap(__float_as_uint(a), __float_as_uint(b), false, false);
    return __uint_as_float(s[0]) + __uint_as_float(s[1]);
}
__device__ __forceinline__ float tsum32(float a, float b) {
    auto s = __builtin_amdgcn_permlane32_swap(__float_as_uint(a), __float_as_uint(b), false, false);
    return __uint_as_float(s[0]) + __uint_as_float(s[1]);
}
__device__ __forceinline__ float sum16(float x) { return tsum16(x, x); }
__device__ __forceinline__ float sum32(float x) { return tsum32(x, x); }
template <int K> __device__ __forceinline__ void fmac_bcast(float& acc, float a, float b) {
    asm("v_fmac_f32_dpp %0, %1, %2 row_newbcast:%3 row_mask:0xf bank_mask:0xf" : "+v"(acc) : "v"(a), "v"(b), "i"(K));
}
template <int K> __device__ __forceinline__ v4u rowbcast(v4u v) {
    typedef long long v2l __attribute__((ext_vector_type(2)));
    v2l x = __builtin_bit_cast(v2l, v), r;
    r[0] = __builtin_amdgcn_update_dpp(x[0], x[0], BCAST + K, 0xf, 0xf, true);
    r[1] = __builtin_amdgcn_update_dpp(x[1], x[1], BCAST + K, 0xf, 0xf, true);
    return __builtin_bit_cast(v4u, r);
}
template <int K> struct Gemv16 { static __device__ __forceinline__ void run(f32x4& acc, v4u a, const v4u (&B)[16]); };
__device__ __forceinline__ float vmax(float a, float b) { float r; asm("v_max_f32 %0, %1, %2" : "=v"(r) : "v"(a), "v"(b)); return r; }
__device__ __forceinline__ float vmax3(float a, float b, float c) { float r; asm("v_max3_f32 %0, %1, %2, %3" : "=v"(r) : "v"(a), "v"(b), "v"(c)); return r; }
__device__ __forceinline__ float max16(float x) {
    auto s = __builtin_amdgcn_permlane16_swap(__float_as_uint(x), __float_as_uint(x), false, false);
    return vmax(__uint_as_float(s[0]), __uint_as_float(s[1]));
}
__device__ __forceinline__ float max32(float x) {
    auto s = __builtin_amdgcn_permlane32_swap(__float_as_uint(x), __float_as_uint(x), false, false);
    return vmax(__uint_as_float(s[0]), __uint_as_float(s[1]));
}
__device__ __forceinline__ float wavesum(float x) {
    x += dpp<XOR1>(x); x += dpp<XOR2>(x); x += dpp<XOR7>(x); x += dpp<ROR8>(x); return sum32(sum16(x));
}
__device__ __forceinline__ float tsum4(f32x4 v) { return tsum32(tsum16(v[0], v[1]), tsum16(v[2], v[3])); }

__device__ __forceinline__ float dot2(unsigned a, unsigned b, float c) {
    return __builtin_amdgcn_fdot2_f32_bf16(__builtin_bit_cast(bf16x2, a), __builtin_bit_cast(bf16x2, b), c, false);
}
__device__ __forceinline__ void psum4(v4u v, float& lo, float& hi) {
    auto a = __builtin_amdgcn_permlane32_swap(v[0], v[2], false, false);
    auto b = __builtin_amdgcn_permlane32_swap(v[1], v[3], false, false);
    auto c = __builtin_amdgcn_permlane16_swap(a[0], b[0], false, false);
    auto d = __builtin_amdgcn_permlane16_swap(a[1], b[1], false, false);
    const unsigned one2 = 0x3f803f80u;
    lo = dot2(__builtin_amdgcn_perm(c[1], c[0], 0x05040100u), one2, 0.f);
    hi = dot2(__builtin_amdgcn_perm(c[1], c[0], 0x07060302u), one2, 0.f);
    lo = dot2(__builtin_amdgcn_perm(d[1], d[0], 0x05040100u), one2, lo);
    hi = dot2(__builtin_amdgcn_perm(d[1], d[0], 0x07060302u), one2, hi);
}
__device__ __forceinline__ v4u ntload(const u16* p) { return __builtin_nontemporal_load((const v4u*)p); }
__device__ __forceinline__ v4u ld16(__amdgpu_buffer_rsrc_t rs, unsigned off) { return __builtin_amdgcn_raw_buffer_load_b128(rs, off, 0, 16); }
__device__ __forceinline__ v2u ld8(__amdgpu_buffer_rsrc_t rs, unsigned off) { return __builtin_amdgcn_raw_buffer_load_b64(rs, off, 0, 16); }
__device__ __forceinline__ unsigned ld4(__amdgpu_buffer_rsrc_t rs, unsigned off) { return __builtin_amdgcn_raw_buffer_load_b32(rs, off, 0, 16); }
__device__ __forceinline__ void st16(__amdgpu_buffer_rsrc_t rs, unsigned off, v4u v) { __builtin_amdgcn_raw_buffer_store_b128(v, rs, off, 0, 16); }
__device__ __forceinline__ void st8(__amdgpu_buffer_rsrc_t rs, unsigned off, v2u v) { __builtin_amdgcn_raw_buffer_store_b64(v, rs, off, 0, 16); }
__device__ __forceinline__ void st4(__amdgpu_buffer_rsrc_t rs, unsigned off, unsigned v) { __builtin_amdgcn_raw_buffer_store_b32(v, rs, off, 0, 16); }
__device__ __forceinline__ unsigned umax4(v4u v) { return max(max(v.x, v.y), max(v.z, v.w)); }
#define MEMBAR() asm volatile("" ::: "memory")

__device__ __forceinline__ bool gate_ok(v2u e, v2u f) {
    u64 tag = (((u64)e.y << 32) | e.x) + 1;
    return __all((((u64)f.y << 32) | f.x) == tag);
}
__device__ __forceinline__ void gate(__amdgpu_buffer_rsrc_t rs, int l) {
    _Pragma("unroll 1") for (unsigned spin = 0; spin < SPIN_CAP; ++spin) {
        MEMBAR();
        v2u e = ld8(rs, OFF_EPOCH);
        v2u f = ld8(rs, OFF_FLAGS + (unsigned)l * 8u);
        if (gate_ok(e, f)) break;
        __builtin_amdgcn_s_sleep(2);
    }
    MEMBAR();
}
__device__ __forceinline__ void gateA(__amdgpu_buffer_rsrc_t rs, int g, int l) {
    _Pragma("unroll 1") for (unsigned spin = 0; spin < SPIN_CAP; ++spin) {
        MEMBAR();
        v2u e = ld8(rs, OFF_EPOCH);
        v2u f = ld8(rs, OFF_AFLAGS + (unsigned)(g * 128 + (l & 15) * 8));
        if (gate_ok(e, f)) break;
        __builtin_amdgcn_s_sleep(2);
    }
    MEMBAR();
}
__device__ __forceinline__ void gate1(__amdgpu_buffer_rsrc_t rs, int l) {
    _Pragma("unroll 1") for (unsigned spin = 0; spin < SPIN_CAP; ++spin) {
        MEMBAR();
        v2u e = ld8(rs, OFF_EPOCH);
        v2u f = ld8(rs, OFF_FLAGS + (unsigned)(l & 31) * 16u);
        if (gate_ok(e, f)) break;
        __builtin_amdgcn_s_sleep(2);
    }
    MEMBAR();
}
__device__ __forceinline__ unsigned now32() { return (unsigned)__builtin_amdgcn_s_memrealtime(); }
__device__ __forceinline__ void announce(__amdgpu_buffer_rsrc_t rs, unsigned off, int ticks) { st4(rs, off, usafe(now32() + (unsigned)ticks)); }
__device__ __forceinline__ void wait_signal(__amdgpu_buffer_rsrc_t rs, unsigned off, int w, int maxwait, unsigned off0 = OOB) {
    if (w == 0) {
        unsigned sg = 0, sg0 = 0;
        if (off0 != OOB) {
            _Pragma("unroll 1") for (unsigned spin = 0; spin < SPIN_CAP; ++spin) {
                MEMBAR();
                sg0 = ld4(rs, off0);
                if (!__any(sg0 == INVALID)) break;
                __builtin_amdgcn_s_sleep(2);
            }
        }
        _Pragma("unroll 1") for (unsigned spin = 0; spin < SPIN_CAP; ++spin) {
            MEMBAR();
            sg = ld4(rs, off);
            if (!__any(sg == INVALID)) break;
        }
        if (off0 != OOB && (int)(sg0 - sg) > 0) sg = sg0;
        const unsigned lim = now32() + (unsigned)maxwait;
        _Pragma("unroll 1") for (;;) {
            const unsigned t = now32();
            if (!__any((int)(sg - t) > 0) || (int)(t - lim) > 0) break;
        }
    }
    __syncthreads();
}
#define KEEP(x) asm volatile("" :: "v"(x))
__device__ __forceinline__ u64 clk() { return __builtin_amdgcn_s_memtime(); }
__device__ __forceinline__ void wait_until(u64 t) { while ((long long)(clk() - t) < 0) __builtin_amdgcn_s_sleep(1); }
#define QLOAD(it_) do { const unsigned go_ = OFF_QP + (unsigned)(it_) * 32768u + goff; qd[0] = ld4(rs, go_); qd[1] = ld4(rs, go_ + 8192u); qd[2] = ld4(rs, go_ + 16384u); qd[3] = ld4(rs, go_ + 24576u); \
                       qs = ld8(rs, soff + (unsigned)(it_) * 1024u); } while (0)
#define CLOAD() do { const unsigned co_ = OFF_CS + goff; cd[0] = ld4(rs, co_); cd[1] = ld4(rs, co_ + 8192u); cd[2] = ld4(rs, co_ + 16384u); cd[3] = ld4(rs, co_ + 24576u); } while (0)
#define BAD2(v) __any(max((v).x, (v).y) == INVALID)
#define PIN8(a) asm volatile("" : "+v"(a[0]), "+v"(a[1]), "+v"(a[2]), "+v"(a[3]), "+v"(a[4]), "+v"(a[5]), "+v"(a[6]), "+v"(a[7]))

__device__ __forceinline__ void tr8_store(const v4u (&in)[8], unsigned char* stg, int C, int kp, int chunk) {
    unsigned char* base = stg + (size_t)(kp * C + 8 * chunk) * 16;
    const int sw = chunk & 7;
#pragma unroll
    for (int dw = 0; dw < 4; ++dw) {
        v4u lo, hi;
#pragma unroll
        for (int kk = 0; kk < 4; ++kk) {
            lo[kk] = __builtin_amdgcn_perm(in[2 * kk + 1][dw], in[2 * kk][dw], 0x05040100u);
            hi[kk] = __builtin_amdgcn_perm(in[2 * kk + 1][dw], in[2 * kk][dw], 0x07060302u);
        }
        *(v4u*)(base + (((2 * dw) ^ sw) << 4)) = lo;
        *(v4u*)(base + (((2 * dw + 1) ^ sw) << 4)) = hi;
    }
}
__device__ __forceinline__ v4u frag_load(const unsigned char* stg, int C, int kp, int ct, int n) {
    const int ch = 2 * ct + (n >> 3);
    return *(const v4u*)(stg + (size_t)(kp * C + 8 * ch + ((n & 7) ^ (ch & 7))) * 16);
}
__device__ __forceinline__ f32x4 mfma(v4u a, v4u b, f32x4 c) {
    return __builtin_amdgcn_mfma_f32_16x16x32_bf16(__builtin_bit_cast(bf16x8, a), __builtin_bit_cast(bf16x8, b), c, 0, 0, 0);
}
#define ZERO4 (f32x4{0.f, 0.f, 0.f, 0.f})
template <int K> __device__ __forceinline__ void Gemv16<K>::run(f32x4& acc, v4u a, const v4u (&B)[16]) {
    acc = mfma(rowbcast<K>(a), B[K], acc);
    if constexpr (K < 15) Gemv16<K + 1>::run(acc, a, B);
}

__global__ void __launch_bounds__(NT, 2)
fused_kernel(const u16* __restrict__ x0, const u16* __restrict__ kc, const u16* __restrict__ vc,
             const u16* __restrict__ wq, const u16* __restrict__ wo, const u16* __restrict__ w1,
             const u16* __restrict__ b1, const u16* __restrict__ w2, const u16* __restrict__ b2,
             u16* __restrict__ out, unsigned char* ws) {
    __shared__ __attribute__((aligned(16))) unsigned char lds0[LDS_BYTES];
    unsigned char* lds = lds0 + LDS_STG;
    const u64 tstart = clk();
    const int b = blockIdx.x, xg = b & 7, bi = b >> 3;
    const int tid = threadIdx.x, w = __builtin_amdgcn_readfirstlane(tid >> 6), l = tid & 63, n = l & 15, g4 = l >> 4;
    asm volatile("" :: "s"(x0), "s"(kc), "s"(vc), "s"(wq), "s"(wo), "s"(w1), "s"(b1), "s"(w2), "s"(b2), "s"(out), "s"(ws));
    const __amdgpu_buffer_rsrc_t rs = __builtin_amdgcn_make_buffer_rsrc(ws, 0, WS_RANGE, 0x00020000);
    unsigned char* sm = lds0;
    const v4u ONES = {0x3f803f80u, 0x3f803f80u, 0x3f803f80u, 0x3f803f80u};
    const v4u Z4 = {0u, 0u, 0u, 0u};

    if (bi < 4) {
        const int c = bi * 8 + xg;
        v2u e = ld8(rs, OFF_EPOCH);
        const v4u inv = {INVALID, INVALID, INVALID, INVALID};
        if (w < 2) {
#pragma unroll
            for (int k = 0; k < 4; ++k) st16(rs, OFF_MB + (unsigned)(w * 131072 + c * 4096 + k * 1024 + l * 16), inv);
            asm volatile("s_waitcnt vmcnt(0)" ::: "memory");
            const u64 tag = (((u64)e.y << 32) | e.x) + 1;
            if (l == 0) st8(rs, OFF_FLAGS + (unsigned)(c * 16 + w * 8), v2u{(unsigned)tag, (unsigned)(tag >> 32)});
        }
    }

    if (bi >= 4 && bi < 12) {
        const int u = bi - 4, g = xg, ks = u >> 1, ns = 2 * g + (u & 1), cb = u;
        v4u Bq[2][4], Bo[16];
        const int xcl = 16 * w + (n & 8) + 2 * g4 + (n & 1);
        const int xc = 128 * ks + xcl;
        u16* xs = (u16*)sm;
        u16* os = (u16*)(sm + 1024);
        v4u in[8];
        unsigned x0r = x0[xc];
        float b2v = 0.f;
        __builtin_amdgcn_sched_barrier(0);
        {
            const u16* p = wq + (size_t)(128 * ks + 32 * (w >> 1) + 8 * g4) * 4096 + 256 * ns + 128 * (w & 1) + 8 * n;
#pragma unroll
            for (int j = 0; j < 8; ++j) in[j] = ntload(p + (size_t)j * 4096);
            __builtin_amdgcn_sched_barrier(0);
            if (tid < 16) *(v4u*)(xs + 192 + 8 * tid) = ONES;
#pragma unroll
            for (int st = 0; st < 4; ++st) { Bq[0][st] = Z4; Bq[1][st] = Z4; }
        }
        const bool qst = (g4 < 2) && !(n & 1);
        const unsigned qcol2 = qst ? (unsigned)(ks * 4096 + 256 * ns + 32 * w + 16 * (g4 & 1) + n) * 2u : OOB - 0x100000u;
        const u16* xa = xs + (n & 1) * 192 + 8 * g4;
        const int yrow = (l & 7) + 8 * g4;
        const unsigned yoff = yrow < 28 ? OFF_YP + (unsigned)((ks * 8 + w) * 1024 + yrow * 32 + (n & 8) * 2) : OOB - 0x100000u;
        const unsigned aoff = OFF_AP + (unsigned)(g * 16 + 4 * g4) * 1024u + (unsigned)(16 * w + n) * 8u;
        const unsigned moff = OFF_ML + (unsigned)(g * 512 + (4 * g4 + (((n >> 2) + u) & 3)) * 32 + (n & 3) * 8);
        const unsigned xpoff = (g4 == 0 && !(n & 1)) ? OFF_XP + (unsigned)((cb * 8 + g) * 64 + 16 * w + n) * 2u : OOB - 0x100000u;
#pragma unroll 1
        for (int it = 0; it < 3; ++it) {
            if (it == 0) {
                asm volatile("" : "+v"(x0r));
                xs[xcl] = (u16)x0r;
                v2u ge = {0u, 0u}, gf = {0u, 0u};
                if (w == 0) { wait_until(tstart + D_G1); MEMBAR(); ge = ld8(rs, OFF_EPOCH); gf = ld8(rs, OFF_FLAGS + (unsigned)(l & 31) * 16u); }
                __builtin_amdgcn_sched_barrier(0);
                PIN8(in);
                tr8_store(in, lds, 256, 4 * (w >> 1) + g4, 16 * (w & 1) + n);
                if (w == 0) { if (!gate_ok(ge, gf)) gate1(rs, l); }
                __syncthreads();
#pragma unroll
                for (int c = 0; c < 2; ++c)
#pragma unroll
                    for (int st = 0; st < 4; ++st) Bq[c][st] = frag_load(lds, 256, 4 * st + g4, 2 * w + c, n);
            }
            if (it > 0) {
                if (it == 2 && ns != 0) return;
                if (it == 1) b2v = bflo(b2[xc]);
                v4u y0; unsigned x1;
                const unsigned yo = yoff + (unsigned)(it - 1) * 32768u, xo = OFF_X1 + (unsigned)((it - 1) * 512 + xc) * 4u;
                wait_signal(rs, OFF_SM + (unsigned)((it - 1) * 4 + (l & 3)) * 128u, w, C_M);
                y0 = ld16(rs, yo); x1 = ld4(rs, xo);
                st4(rs, (it == 1 && tid == 448) ? OFF_SQ + (unsigned)(g * 8 + u) * 64u : OOB, usafe(now32() + C_Q));
                __builtin_amdgcn_sched_barrier(0);
                if (__any(max(umax4(y0), x1) == INVALID)) {
                    _Pragma("unroll 1") for (unsigned spin = 0; spin < SPIN_CAP; ++spin) {
                        __builtin_amdgcn_s_sleep(RETRY);
                        MEMBAR();
                        y0 = ld16(rs, yo); x1 = ld4(rs, xo);
                        if (!__any(max(umax4(y0), x1) == INVALID)) break;
                    }
                }
                float lo, hi;
                psum4(y0, lo, hi);
                lo += dpp<XOR1>(lo); hi += dpp<XOR1>(hi); lo += dpp<XOR2>(lo); hi += dpp<XOR2>(hi); lo += dpp<XOR7>(lo); hi += dpp<XOR7>(hi);
                const float t = (n & 1) ? hi : lo;
                const float xv = rbf(__uint_as_float(x1) + rbf(rbf(t) + b2v));
                if (it == 2) {
                    if (!(n & 6)) out[xc] = (u16)(__float_as_uint(xv) >> 16);
                    if (ks == 0 && tid == 0) {
                        v2u e = ld8(rs, OFF_EPOCH);
                        u64 tag = (((u64)e.y << 32) | e.x) + 1;
                        st8(rs, OFF_EPOCH, v2u{(unsigned)tag, (unsigned)(tag >> 32)});
                    }
                    return;
                }
                xs[xcl] = (u16)(__float_as_uint(xv) >> 16);
                if (ns == 0) st4(rs, !(n & 6) ? OFF_X2 + (unsigned)xc * 4u : OOB, fsafe(xv));
                __syncthreads();
            }
            {
                f32x4 a0 = ZERO4, a1 = ZERO4;
                v4u A[4];
#pragma unroll
                for (int st = 0; st < 4; ++st) A[st] = *(const v4u*)(xa + 32 * st);
                const unsigned xw = *(const unsigned*)(xs + 2 * l);
#pragma unroll
                for (int st = 0; st < 4; ++st) { a0 = mfma(A[st], Bq[0][st], a0); a1 = mfma(A[st], Bq[1][st], a1); }
                const float e0 = bflo(xw), e1 = bfhi(xw);
                const float mean = wavesum(e0 + e1) * (1.f / 128.f);
                const float csv = (g4 & 1) ? a1[1] : a0[1];
                const float qv = fmaf(-mean, csv, (g4 & 1) ? a1[0] : a0[0]);
                const unsigned qw = usafe(pack2(qv, dpp<XOR1>(qv))), cw = usafe(pack2(csv, dpp<XOR1>(csv)));
                st4(rs, OFF_QP + (unsigned)it * 32768u + qcol2, qw);
                if (w == 0 && !(u & 1)) {
                    const float d0 = e0 - mean, d1 = e1 - mean;
                    const float m2 = wavesum(d0 * d0 + d1 * d1);
                    st8(rs, l == 0 ? OFF_QS + (unsigned)((it * 8 + g) * 128 + ks * 32) : OOB, v2u{fsafe(mean), fsafe(m2)});
                }
                if (it == 0) st4(rs, OFF_CS + qcol2, cw);
            }
            if (it == 0) {
                __syncthreads();
                wait_until(tstart + D_WO);
                const u16* p = wo + (size_t)(512 * g + 64 * w + 32 * (n >> 3) + 8 * g4) * 512 + 64 * cb + 8 * (n & 7);
#pragma unroll
                for (int j = 0; j < 8; ++j) in[j] = ntload(p + (size_t)j * 512);
                MEMBAR();
                const v2u ge = ld8(rs, w < 2 ? OFF_EPOCH : OOB);
                const v2u gf = ld8(rs, w == 0 ? OFF_FLAGS + (unsigned)l * 8u : w == 1 ? OFF_AFLAGS + (unsigned)(g * 128 + n * 8) : OOB);
                __builtin_amdgcn_sched_barrier(0);
                tr8_store(in, lds, 64, 8 * w + 4 * (n >> 3) + g4, n & 7);
                if (w < 2 && !gate_ok(ge, gf)) { if (w == 0) gate(rs, l); else gateA(rs, g, l); }
                __syncthreads();
                if (tid == 448) announce(rs, OFF_SR + (unsigned)(g * 128 + u * 16), C_W + 45);
                if (w < 4) {
#pragma unroll
                    for (int st = 0; st < 16; ++st) Bo[st] = frag_load(lds, 64, 4 * st + g4, w & 3, n);
                }
            }
            wait_signal(rs, OFF_SA + (unsigned)((it * 8 + g) * 16 + n) * 64u, w - 4, C_A);
            {
                v2u a[4]; v2u ml;
                const unsigned ao = aoff + (unsigned)it * 131072u, mo = moff + (unsigned)it * 4096u;
                ml = ld8(rs, mo);
#pragma unroll
                for (int i = 0; i < 4; ++i) a[i] = ld8(rs, ao + (unsigned)((i + u) & 3) * 1024u);
                unsigned tis = now32();
                __builtin_amdgcn_sched_barrier(0);
                if (BAD2(ml)) {
                    _Pragma("unroll 1") for (unsigned spin = 0; spin < SPIN_CAP; ++spin) {
                        __builtin_amdgcn_s_sleep(RETRY);
                        MEMBAR();
                        ml = ld8(rs, mo);
                        tis = now32();
                        if (!BAD2(ml)) break;
                    }
                }
                st4(rs, (tid == 448 && u == 0) ? OFF_SW + (unsigned)(it * 8 + g) * 128u : OOB, usafe(tis + C_W));
                __builtin_amdgcn_sched_barrier(0);
                const float mm = __uint_as_float(ml.x);
                float M = vmax(mm, dpp<ROR4>(mm)); M = vmax(M, dpp<ROR8>(M)); M = max32(max16(M));
                const float e = __builtin_amdgcn_exp2f(mm - M);
                float L = e * __uint_as_float(ml.y); L += dpp<ROR4>(L); L += dpp<ROR8>(L); L = sum32(sum16(L));
                float al = e * __builtin_amdgcn_rcpf(L);
                asm volatile("" : "+v"(al));
                __builtin_amdgcn_sched_barrier(0);
                if (__any(max(max(max(a[0].x, a[0].y), max(a[1].x, a[1].y)), max(max(a[2].x, a[2].y), max(a[3].x, a[3].y))) == INVALID)) {
                    _Pragma("unroll 1") for (unsigned spin = 0; spin < SPIN_CAP; ++spin) {
                        const bool n0 = BAD2(a[0]), n1 = BAD2(a[1]), n2 = BAD2(a[2]), n3 = BAD2(a[3]);
                        if (!(n0 | n1 | n2 | n3)) break;
                        __builtin_amdgcn_s_sleep(RETRY);
                        MEMBAR();
                        if (n0) a[0] = ld8(rs, ao + (unsigned)(u & 3) * 1024u);
                        if (n1) a[1] = ld8(rs, ao + (unsigned)((1 + u) & 3) * 1024u);
                        if (n2) a[2] = ld8(rs, ao + (unsigned)((2 + u) & 3) * 1024u);
                        if (n3) a[3] = ld8(rs, ao + (unsigned)((3 + u) & 3) * 1024u);
                    }
                    asm volatile("s_waitcnt vmcnt(0)" : "+v"(a[0]), "+v"(a[1]), "+v"(a[2]), "+v"(a[3]));
                }
                f32x4 o;
                o[0] = dpp<BCAST + 0>(al) * bflo(a[0].x); o[1] = dpp<BCAST + 1>(al) * bfhi(a[0].x);
                o[2] = dpp<BCAST + 2>(al) * bflo(a[0].y); o[3] = dpp<BCAST + 3>(al) * bfhi(a[0].y);
                float o0 = o[0], o1 = o[1], o2 = o[2], o3 = o[3];
                fmac_bcast<4>(o0, al, bflo(a[1].x)); fmac_bcast<5>(o1, al, bfhi(a[1].x)); fmac_bcast<6>(o2, al, bflo(a[1].y)); fmac_bcast<7>(o3, al, bfhi(a[1].y));
                fmac_bcast<8>(o0, al, bflo(a[2].x)); fmac_bcast<9>(o1, al, bfhi(a[2].x)); fmac_bcast<10>(o2, al, bflo(a[2].y)); fmac_bcast<11>(o3, al, bfhi(a[2].y));
                fmac_bcast<12>(o0, al, bflo(a[3].x)); fmac_bcast<13>(o1, al, bfhi(a[3].x)); fmac_bcast<14>(o2, al, bflo(a[3].y)); fmac_bcast<15>(o3, al, bfhi(a[3].y));
                asm volatile("s_nop 1" : "+v"(o0), "+v"(o1), "+v"(o2), "+v"(o3));
                o = f32x4{o0, o1, o2, o3};
                const float ov = tsum4(o);
                os[128 * g4 + 16 * w + n] = (u16)pack2(ov, ov);
                __syncthreads();
                if (w < 4) {
                    f32x4 acc = ZERO4;
                    Gemv16<0>::run(acc, *(const v4u*)(os + 32 * n + 8 * g4), Bo);
                    st4(rs, xpoff + (unsigned)it * 8192u, usafe(pack2(acc[0], dpp<XOR1>(acc[0]))));
                }
            }
        }
        return;
    }

    if (bi >= 12 && bi < 28) {
        const int g = xg, s = bi - 12;
        const size_t base = ((size_t)g * SK + (SK - WIN) + 256 * s + 32 * w) * DH + g4 * DH + 8 * n;
        v4u Kf[2][4], Vf[8];
        float* mxs = (float*)sm;
        float* lss = (float*)(sm + 256);
        unsigned* Ps = (unsigned*)(sm + 1024);
        unsigned char* qim = lds0 + 10240;
        const int h = n >> 2;
        const bool lead = (g4 == 0) && ((n & 3) == 0);
        const int sidx = lead ? h * 8 + w : 40;
        const int lidx = (n & 3) == 0 ? (h * 8 + w) * 4 + g4 : 130;
        const int pidx = ((w * 4 + h) * 4 + g4) * 4 + 2 * (n & 1) + ((n >> 1) & 1);
        const unsigned goff = w < 4 ? (unsigned)(512 * g + 128 * w + 2 * l) * 2u : OOB - 0x200000u;
        const unsigned soff = w < 4 ? OFF_QS + (unsigned)(g * 128 + (l & 3) * 32) : OOB - 0x200000u;
        const int gr = h * 272 + 16 * g4;
        const unsigned apoff = g4 == 0 ? OFF_AP + (unsigned)(g * 16 + s) * 1024u + (unsigned)(16 * w + n) * 8u : OOB - 0x100000u;
        v4u qd = Z4, cd = Z4; v2u qs = {0u, 0u};
        {
            v4u Vr[8], Kr[8];
            const v2u ge = ld8(rs, w == 0 ? OFF_EPOCH : OOB);
            {
                const unsigned co = w == 0 ? OFF_AP + (unsigned)((g * 16 + s) * 1024 + l * 16) : OOB - 0x200000u;
                st16(rs, co, v4u{INVALID, INVALID, INVALID, INVALID});
                st16(rs, co + 131072u, v4u{INVALID, INVALID, INVALID, INVALID});
            }
            __builtin_amdgcn_sched_barrier(0);
#pragma unroll
            for (int jj = 0; jj < 8; ++jj) Vr[jj] = ntload(vc + base + 4 * jj * DH);
            __builtin_amdgcn_sched_barrier(0);
            __builtin_amdgcn_s_barrier();
            __builtin_amdgcn_sched_barrier(0);
#pragma unroll
            for (int jj = 0; jj < KSPLIT; ++jj) Kr[jj] = ntload(kc + base + 4 * jj * DH);
            __builtin_amdgcn_sched_barrier(0);
            asm volatile("s_waitcnt vmcnt(%0)" :: "i"(8 + KSPLIT) : "memory");
            if (w == 0) wait_until(tstart + D_GA);
            { const u64 tag = (((u64)ge.y << 32) | ge.x) + 1; st8(rs, tid == 0 ? OFF_AFLAGS + (unsigned)(g * 128 + s * 8) : OOB, v2u{(unsigned)tag, (unsigned)(tag >> 32)}); }
            MEMBAR();
            const v2u gf = ld8(rs, w == 0 ? OFF_FLAGS + (unsigned)l * 8u : OOB);
            __builtin_amdgcn_sched_barrier(0);
#pragma unroll
            for (int jj = KSPLIT; jj < 8; ++jj) Kr[jj] = ntload(kc + base + 4 * jj * DH);
            __builtin_amdgcn_sched_barrier(0);
            tr8_store(Vr, lds, 128, 4 * w + g4, n);
            if (w == 0) { if (!gate_ok(ge, gf)) gate(rs, l); }
            __syncthreads();
#pragma unroll
            for (int kb = 0; kb < 8; ++kb) Vf[kb] = frag_load(lds, 128, 4 * kb + g4, w, n);
            MEMBAR();
            unsigned char* kst = lds + 65536 + w * 8192;
            if (w < 4) wait_until(tstart + D_Q1);
            QLOAD(0); CLOAD();
            __builtin_amdgcn_sched_barrier(0);
#pragma unroll
            for (int jj = 0; jj < 8; ++jj) *(v4u*)(kst + (4 * jj + g4) * 256 + ((n ^ (4 * g4 + (jj & 3))) << 4)) = Kr[jj];
#pragma unroll
            for (int t = 0; t < 2; ++t)
#pragma unroll
                for (int st = 0; st < 4; ++st) Kf[t][st] = *(const v4u*)(kst + (16 * t + 4 * (n & 3) + (n >> 2)) * 256 + (((4 * st + g4) ^ n) << 4));
        }
#pragma unroll 1
        for (int it = 0; it < 2; ++it) {
            if (it == 1) {
                wait_signal(rs, OFF_SQ + (unsigned)(g * 8 + (l & 7)) * 64u, w, C_Q);
                if (w < 4) QLOAD(1);
            }
            if (w < 4) {
                _Pragma("unroll 1") for (unsigned spin = 0; spin < SPIN_CAP; ++spin) {
                    if (!__any(max(max(umax4(qd), umax4(cd)), max(qs.x, qs.y)) == INVALID)) break;
                    __builtin_amdgcn_s_sleep(RETRY);
                    MEMBAR();
                    QLOAD(it);
                    if (it == 0) CLOAD();
                }
                const float mk = __uint_as_float(qs.x);
                float mean = mk + dpp<XOR1>(mk); mean += dpp<XOR2>(mean); mean *= 0.25f;
                const float delta = mk - rbf(mean);
                const unsigned one2 = 0x3f803f80u;
                float lo = dot2(__builtin_amdgcn_perm(qd[1], qd[0], 0x05040100u), one2, 0.f);
                float hi = dot2(__builtin_amdgcn_perm(qd[1], qd[0], 0x07060302u), one2, 0.f);
                lo = dot2(__builtin_amdgcn_perm(qd[3], qd[2], 0x05040100u), one2, lo);
                hi = dot2(__builtin_amdgcn_perm(qd[3], qd[2], 0x07060302u), one2, hi);
                lo = fmaf(dpp<0x00>(delta), bflo(cd[0]), lo); hi = fmaf(dpp<0x00>(delta), bfhi(cd[0]), hi);
                lo = fmaf(dpp<0x55>(delta), bflo(cd[1]), lo); hi = fmaf(dpp<0x55>(delta), bfhi(cd[1]), hi);
                lo = fmaf(dpp<0xAA>(delta), bflo(cd[2]), lo); hi = fmaf(dpp<0xAA>(delta), bfhi(cd[2]), hi);
                lo = fmaf(dpp<0xFF>(delta), bflo(cd[3]), lo); hi = fmaf(dpp<0xFF>(delta), bfhi(cd[3]), hi);
                *(unsigned*)(qim + w * 272 + 4 * l) = pack2(lo, hi);
                if (w == 0) {
                    const float dm = mk - mean;
                    float m2 = fmaf(128.f * dm, dm, __uint_as_float(qs.y)); m2 += dpp<XOR1>(m2); m2 += dpp<XOR2>(m2);
                    mxs[48] = m2;
                }
            }
            __syncthreads();
            if (tid == 448) announce(rs, OFF_SA + (unsigned)((it * 8 + g) * 16 + s) * 64u, C_A);
            v4u Q[4];
#pragma unroll
            for (int st = 0; st < 4; ++st) Q[st] = *(const v4u*)(qim + gr + 64 * st);
            f32x4 D0 = ZERO4, D1 = ZERO4;
#pragma unroll
            for (int st = 0; st < 4; ++st) { D0 = mfma(Kf[0][st], Q[st], D0); D1 = mfma(Kf[1][st], Q[st], D1); }
            const float sc = __builtin_amdgcn_rcpf(rbf(__builtin_amdgcn_sqrtf(rbf(rbf(mxs[48] * (1.f / 512.f)) + 1e-5f)))) * (0.08838834764831845f * 1.4426950408889634f);
            KEEP(sc);
            float z[2];
#pragma unroll
            for (int i = 0; i < 2; ++i) {
                const float a0 = (n & 2) ? D0[2 + i] : D0[i], a1 = (n & 2) ? D1[2 + i] : D1[i];
                z[i] = (n & 1) ? a1 : a0;
            }
            float mx = vmax(z[0], z[1]);
            mx = vmax(mx, dpp<XOR1>(mx)); mx = vmax(mx, dpp<XOR2>(mx)); mx = max32(max16(mx));
            mxs[sidx] = mx;
            __syncthreads();
            const f32x4 ma = *(const f32x4*)(mxs + h * 8), mb_ = *(const f32x4*)(mxs + h * 8 + 4);
            const float M = vmax3(vmax3(ma[0], ma[1], ma[2]), vmax3(ma[3], mb_[0], mb_[1]), vmax(mb_[2], mb_[3]));
            const float Msc = M * sc;
            const float p0 = __builtin_amdgcn_exp2f(fmaf(z[0], sc, -Msc)), p1 = __builtin_amdgcn_exp2f(fmaf(z[1], sc, -Msc));
            Ps[pidx] = pack2(p0, p1);
            { float ls = p0 + p1; ls += dpp<XOR1>(ls); ls += dpp<XOR2>(ls); lss[lidx] = ls; }
            __syncthreads();
            v4u Pf[8];
#pragma unroll
            for (int kb = 0; kb < 8; ++kb) Pf[kb] = *(const v4u*)(Ps + ((kb * 4 + (n & 3)) * 4 + g4) * 4);
            __builtin_amdgcn_sched_barrier(0);
            f32x4 acc = ZERO4;
            if (w == 7) {
                const v2u lp = *(const v2u*)(lss + 2 * l);
                float L = __uint_as_float(lp.x) + __uint_as_float(lp.y);
                L += dpp<XOR1>(L); L += dpp<XOR2>(L); L += dpp<XOR7>(L); L += dpp<ROR8>(L);
                st8(rs, n == 4 * g4 ? OFF_ML + (unsigned)(((it * 8 + g) * 16 + s) * 32 + g4 * 8) : OOB, v2u{fsafe(Msc), fsafe(L)});
            }
#pragma unroll
            for (int kb = 0; kb < 8; ++kb) acc = mfma(Pf[kb], Vf[kb], acc);
            st8(rs, apoff + (unsigned)it * 131072u, v2u{usafe(pack2(acc[0], acc[1])), usafe(pack2(acc[2], acc[3]))});
        }
        return;
    }

    {
        const int m = (bi < 4 ? 32 + bi * 8 : (bi - 28) * 8) + xg;
        if (m >= 56) return;
        const int half = m >= 28, p = m - 28 * half;
        v4u B1[16], B2[2][2];
        u16* xs = (u16*)sm;
        u16* hs = (u16*)(sm + 2560);
        float* sts = (float*)(sm + 3072);
        const int stidx = (l & 31) == 0 ? (l >> 5) * 8 + w : 20;
        const int xc = 64 * w + 8 * (n & 7) + 2 * g4 + (n >> 3);
        const int hc = 16 * (w & 3) + n;
        const float x0v = bflo(x0[xc]), b1v = bflo(b1[64 * p + hc]);
        {
            if (w == 0) { wait_until(tstart + D_G1); gate(rs, l); }
            wait_until(tstart + D_ML);
            v4u in[8], in2[8];
            const u16* p1 = w1 + (size_t)(64 * w + 32 * (n >> 3) + 8 * g4) * FF + 64 * p + 8 * (n & 7);
#pragma unroll
            for (int j = 0; j < 8; ++j) in[j] = ntload(p1 + (size_t)j * FF);
            if (w >= 4) {
                const u16* p2 = w2 + (size_t)(64 * p + 32 * ((w >> 1) & 1) + 8 * g4) * 512 + 256 * half + 128 * (w & 1) + 8 * n;
#pragma unroll
                for (int j = 0; j < 8; ++j) in2[j] = ntload(p2 + (size_t)j * 512);
            }
            __builtin_amdgcn_sched_barrier(0);
            tr8_store(in, lds, 64, 8 * w + 4 * (n >> 3) + g4, n & 7);
            if (w >= 4) tr8_store(in2, lds + 65536, 256, 4 * ((w >> 1) & 1) + g4, 16 * (w & 1) + n);
            __syncthreads();
#pragma unroll
            for (int st = 0; st < 16; ++st) B1[st] = frag_load(lds, 64, 4 * st + g4, w & 3, n);
#pragma unroll
            for (int c = 0; c < 2; ++c)
#pragma unroll
                for (int st = 0; st < 2; ++st) B2[c][st] = frag_load(lds + 65536, 256, 4 * st + g4, 2 * w + c, n);
        }
        float cs1;
        {
            f32x4 a = ZERO4;
#pragma unroll
            for (int st = 0; st < 16; ++st) a = mfma(ONES, B1[st], a);
            cs1 = a[0];
        }
        const unsigned xoff = OFF_XP + (unsigned)(w * 1024 + l * 16);
        const unsigned ypoff = (g4 < 2 && !(n & 1)) ? OFF_YP + (unsigned)((16 * half + 2 * w + g4) * 1024 + p * 32 + n * 2) : OOB - 0x100000u;
#pragma unroll 1
        for (int it = 0; it < 2; ++it) {
            v4u a0; unsigned xp;
            const unsigned xo = xoff + (unsigned)it * 8192u, po = it == 0 ? OOB : OFF_X2 + (unsigned)xc * 4u;
            wait_signal(rs, OFF_SW + (unsigned)(it * 8 + (l & 7)) * 128u, w, C_W + 45, it == 0 ? OFF_SR + (unsigned)((l & 7) * 128 + (l >> 3) * 16) : OOB);
            unsigned tis = 0;
            _Pragma("unroll 1") for (unsigned spin = 0; spin < SPIN_CAP; ++spin) {
                MEMBAR();
                a0 = ld16(rs, xo); xp = ld4(rs, po);
                tis = now32();
                if (!__any(max(umax4(a0), xp) == INVALID)) break;
                __builtin_amdgcn_s_sleep(RETRY);
            }
            asm volatile("" : "+v"(a0), "+v"(xp));
            if (w == 7 && (m & 15) == 3) st4(rs, l == 0 ? OFF_SM + (unsigned)(it * 4 + (m >> 4)) * 128u : OOB, usafe(tis + C_M));
            const float xprev = it == 1 ? __uint_as_float(xp) : x0v;
            float lo, hi;
            psum4(a0, lo, hi);
            lo += dpp<ROR8>(lo); hi += dpp<ROR8>(hi);
            const float xv = rbf(xprev + rbf((n & 8) ? hi : lo));
            xs[xc] = (u16)(__float_as_uint(xv) >> 16);
            {
                float t = tsum32(xv, xv * xv);
                t += dpp<XOR1>(t); t += dpp<XOR2>(t); t += dpp<XOR7>(t); t += dpp<ROR8>(t); t = sum16(t);
                sts[stidx] = t;
            }
            if (m == w) st4(rs, OFF_X1 + (unsigned)(it * 512 + xc) * 4u, fsafe(xv));
            __syncthreads();
            if (w < 4) {
                f32x4 acc = ZERO4;
                const v4u Ax = *(const v4u*)(xs + 32 * n + 8 * g4);
                const f32x4 s0 = *(const f32x4*)sts, s1 = *(const f32x4*)(sts + 4), q0 = *(const f32x4*)(sts + 8), q1 = *(const f32x4*)(sts + 12);
                Gemv16<0>::run(acc, Ax, B1);
                const f32x4 sa = s0 + s1, qa = q0 + q1;
                const float mean = ((sa[0] + sa[1]) + (sa[2] + sa[3])) * (1.f / 512.f);
                const float var = fmaxf(fmaf(-mean, mean, ((qa[0] + qa[1]) + (qa[2] + qa[3])) * (1.f / 512.f)), 0.f);
                const float mb = rbf(mean);
                const float rden = __builtin_amdgcn_rcpf(rbf(__builtin_amdgcn_sqrtf(rbf(rbf(var) + 1e-5f))));
                const float hv = rbf(rbf(fmaf(-mb, cs1, acc[0]) * rden) + b1v);
                const float uu = hv * fmaf(hv * hv, 0.044715f * 2.302208198f, 2.302208198f);
                const float gl = hv - hv * __builtin_amdgcn_rcpf(__builtin_amdgcn_exp2f(uu) + 1.f);
                hs[hc] = (u16)pack2(gl, gl);
            }
            __syncthreads();
            {
                const v4u H0 = *(const v4u*)(hs + 8 * g4), H1 = *(const v4u*)(hs + 32 + 8 * g4);
                f32x4 acc[2];
#pragma unroll
                for (int c = 0; c < 2; ++c) acc[c] = mfma(H0, B2[c][0], ZERO4);
#pragma unroll
                for (int c = 0; c < 2; ++c) acc[c] = mfma(H1, B2[c][1], acc[c]);
                const float yv = (g4 & 1) ? acc[1][0] : acc[0][0];
                st4(rs, ypoff + (unsigned)it * 32768u, usafe(pack2(yv, dpp<XOR1>(yv))));
            }
        }
    }
}

}

extern "C" void kernel_launch(void* const* d_in, const int* in_sizes, int n_in,
                              void* d_out, int out_size, void* d_ws, size_t ws_size,
                              hipStream_t stream) {
    fused_kernel<<<256, NT, 0, stream>>>((const u16*)d_in[0], (const u16*)d_in[1], (const u16*)d_in[2],
                                         (const u16*)d_in[3], (const u16*)d_in[4], (const u16*)d_in[6],
                                         (const u16*)d_in[7], (const u16*)d_in[8], (const u16*)d_in[9],
                                         (u16*)d_out, (unsigned char*)d_ws);
}
```
